```python
import torch

def _softmax(x, axis=-1):
    return torch.softmax(x, dim=axis)
def _gelu(x):
    return torch.nn.functional.gelu(x, approximate="tanh")
def _silu(x):
    return torch.nn.functional.silu(x)
def _sigmoid(x):
    return torch.sigmoid(x)
def _layernorm(x, axis=-1, eps=1e-5):
    m = x.mean(dim=axis, keepdim=True)
    v = x.var(dim=axis, keepdim=True, unbiased=False)
    return (x - m) / torch.sqrt(v + eps)
def _scatter_add(tgt, idx, src, axis=0):
    return tgt.clone().index_add_(axis, idx, src)
def _segment_sum(src, seg, n):
    out = torch.zeros((n,) + src.shape[1:], dtype=src.dtype)
    return out.index_add_(0, seg, src)
def _pad(x, pw):
    flat = tuple(v for pair in reversed(pw) for v in pair)
    return torch.nn.functional.pad(x, flat)

kernel_name = 'recurrent_scan__ssm_conv_h3__dense_activation_silu__prompt'

def setup_inputs():

    _w = (lambda a: (lambda t: t.to(torch.bfloat16) if t.is_floating_point() else t)(torch.as_tensor(a)))
    def _A_setup():
        B, S, D = 1, 2048, 576
        ks = 4
        return {
            "q": _w((lambda *s: torch.randn(*s, dtype=torch.bfloat16) * 0.05)(B, S, D)),
            "k": _w((lambda *s: torch.randn(*s, dtype=torch.bfloat16) * 0.8)(B, S, D)),
            "v": _w((lambda *s: torch.randn(*s, dtype=torch.bfloat16) * 0.8)(B, S, D)),
            "shift_k": _w((lambda *s: torch.randn(*s, dtype=torch.bfloat16) * 0.8)(D, ks) * 0.5),
            "ssm_k": _w((lambda *s: torch.randn(*s, dtype=torch.bfloat16) * 0.8)(D, S) * (1.0 / S ** 0.5)),
            "ssm_D": _w((lambda *s: torch.randn(*s, dtype=torch.bfloat16) * 0.8)(D)),
        }
    _A_in = _A_setup()

    def _B_setup():
        B, S, D, F = 1, 2048, 576, 4608
        return {
            "x": _w((lambda *s: torch.randn(*s, dtype=torch.bfloat16) * 0.05)(B, S, D)),
            "w1": _w((lambda *s: torch.randn(*s, dtype=torch.bfloat16) * 0.8)(D, F) * (D ** -0.5)),
            "w2": _w((lambda *s: torch.randn(*s, dtype=torch.bfloat16) * 0.2)(F, D) * (F ** -0.5)),
        }
    _B_in = _B_setup()

    return {
        "A_q": _A_in["q"],
        "A_k": _A_in["k"],
        "A_v": _A_in["v"],
        "A_shift_k": _A_in["shift_k"],
        "A_ssm_k": _A_in["ssm_k"],
        "A_ssm_D": _A_in["ssm_D"],
        "B_x": _B_in["x"],
        "B_w1": _B_in["w1"],
        "B_w2": _B_in["w2"],
    }

def reference(A_q, A_k, A_v, A_shift_k, A_ssm_k, A_ssm_D, B_x, B_w1, B_w2):
    def _A_ref(q, k, v, shift_k, ssm_k, ssm_D):
        B, S, D = q.shape
        ks = shift_k.shape[-1]
        k_pad = _pad(k, ((0, 0), (ks - 1, 0), (0, 0)))
        k_shift = torch.zeros_like(k)
        for t in range(ks):
            k_shift = k_shift + k_pad[:, t:t + S, :] * shift_k[:, ks - 1 - t][None, None, :]
        kv = k_shift * v
        L = 2 * S
        kv_pad = _pad(kv, ((0, 0), (0, S), (0, 0)))
        kf_pad = _pad(ssm_k, ((0, 0), (0, S)))
        kv_f = (lambda x, axes=(-2,-1): torch.fft.rfft2(x.float(), dim=axes))(kv_pad[:, None, :, :], axes=(1, 2))
        kf_f = (lambda x, axes=(-2,-1): torch.fft.rfft2(x.float(), dim=axes))(kf_pad[:, None, :], axes=(1, 2))
        kf_f = (lambda x, *d: x.permute(*d))(kf_f, 1, 2, 0)
        y_f = kv_f * kf_f[None, :, :, :]
        y = (lambda x, s, axes=(-2,-1): torch.fft.irfft2(x, s=s, dim=axes).to(torch.bfloat16))(y_f, (1, L), axes=(1, 2))[:, 0, :S, :]
        y = y + kv * ssm_D[None, None, :]
        return q * y

    def _B_ref(x, w1, w2):
        h = torch.einsum("bsd,df->bsf", x, w1)
        h = _silu(h)
        out = torch.einsum("bsf,fd->bsd", h, w2)
        return out

    x = A_q
    xn = _layernorm(x, axis=-1)
    s = _A_ref(xn, A_k, A_v, A_shift_k, A_ssm_k, A_ssm_D)
    g = _silu(_B_ref(xn, B_w1, B_w2))
    return x + s * g
```

```cpp
#include <hip/hip_runtime.h>
#include <hip/hip_bf16.h>
#include <stdint.h>

typedef unsigned short u16;
__device__ __forceinline__ float bf2f(u16 v) { return __uint_as_float(((unsigned)v) << 16); }
__device__ __forceinline__ u16 f2bf(float f) { __hip_bfloat16 b = __float2bfloat16(f); return *(u16*)&b; }
__device__ __forceinline__ float rbf(float f) { return bf2f(f2bf(f)); }

constexpr int S = 2048, D = 576, F = 4608;

__global__ void k_ln(const u16* x, u16* xn) {
  int t = blockIdx.x; int lane = threadIdx.x;
  float v[9]; float s = 0;
  for (int i = 0; i < 9; ++i) { v[i] = bf2f(x[t * D + lane + 64 * i]); s += v[i]; }
  for (int o = 32; o; o >>= 1) s += __shfl_xor(s, o);
  float m = s / D; float q = 0;
  for (int i = 0; i < 9; ++i) { float d = v[i] - m; q += d * d; }
  for (int o = 32; o; o >>= 1) q += __shfl_xor(q, o);
  float var = rbf(q / D); float mb = rbf(m);
  float sd = rbf(sqrtf(rbf(var + 1e-5f)));
  for (int i = 0; i < 9; ++i) xn[t * D + lane + 64 * i] = f2bf(rbf(v[i] - mb) / sd);
}
__global__ void k_kv(const u16* k, const u16* v, const u16* sk, u16* kv) {
  int i = blockIdx.x * blockDim.x + threadIdx.x; if (i >= S * D) return;
  int t = i / D, d = i % D; float acc = 0;
  for (int j = 0; j < 4; ++j) { int tt = t + j - 3; float kk = tt >= 0 ? bf2f(k[tt * D + d]) : 0.f;
    acc = rbf(acc + rbf(kk * bf2f(sk[d * 4 + 3 - j]))); }
  kv[i] = f2bf(acc * bf2f(v[i]));
}
__global__ void k_conv(const u16* kv, const u16* h, const u16* Dv, const u16* xn, u16* s) {
  int i = blockIdx.x * blockDim.x + threadIdx.x; if (i >= S * D) return;
  int t = i / D, d = i % D; float acc = 0;
  for (int j = 0; j <= t; ++j) acc += bf2f(kv[j * D + d]) * bf2f(h[d * S + t - j]);
  float y = rbf(acc); y = rbf(y + rbf(bf2f(kv[i]) * bf2f(Dv[d])));
  s[i] = f2bf(bf2f(xn[i]) * y);
}
__global__ void k_g1(const u16* xn, const u16* w1, u16* H) {
  int f = blockIdx.x * blockDim.x + threadIdx.x; int t = blockIdx.y; float acc = 0;
  for (int k = 0; k < D; ++k) acc += bf2f(xn[t * D + k]) * bf2f(w1[k * F + f]);
  float h = rbf(acc); H[(size_t)t * F + f] = f2bf(h / (1.f + __expf(-h)));
}
__global__ void k_g2(const u16* H, const u16* w2, const u16* x, const u16* s, u16* out) {
  int d = blockIdx.x * blockDim.x + threadIdx.x; int t = blockIdx.y; float acc = 0;
  for (int f = 0; f < F; ++f) acc += bf2f(H[(size_t)t * F + f]) * bf2f(w2[f * D + d]);
  float o = rbf(acc); float g = rbf(o / (1.f + __expf(-o)));
  out[t * D + d] = f2bf(bf2f(x[t * D + d]) + rbf(bf2f(s[t * D + d]) * g));
}

extern "C" void kernel_launch(void* const* d_in, const int* in_sizes, int n_in, void* d_out, int out_size,
                              void* d_ws, size_t ws_size, hipStream_t stream) {
  const u16* q = (const u16*)d_in[0]; const u16* k = (const u16*)d_in[1]; const u16* v = (const u16*)d_in[2];
  const u16* sk = (const u16*)d_in[3]; const u16* h = (const u16*)d_in[4]; const u16* Dv = (const u16*)d_in[5];
  const u16* w1 = (const u16*)d_in[7]; const u16* w2 = (const u16*)d_in[8];
  u16* ws = (u16*)d_ws; u16* xn = ws; u16* kv = xn + S * D; u16* s = kv + S * D; u16* H = s + S * D;
  k_ln<<<S, 64, 0, stream>>>(q, xn);
  k_kv<<<S * D / 256, 256, 0, stream>>>(k, v, sk, kv);
  k_conv<<<S * D / 256, 256, 0, stream>>>(kv, h, Dv, xn, s);
  k_g1<<<dim3(F / 256, S), 256, 0, stream>>>(xn, w1, H);
  k_g2<<<dim3(D / 64, S), 64, 0, stream>>>(H, w2, q, s, (u16*)d_out);
}
```

```cpp
#include <hip/hip_runtime.h>
#include <hip/hip_bf16.h>
#include <stdint.h>
#include <utility>

typedef unsigned short u16;
typedef unsigned int u32;
typedef __attribute__((address_space(3))) void* lds_ptr_t;
typedef __attribute__((ext_vector_type(8))) __bf16 bf16x8;
typedef __attribute__((ext_vector_type(2))) __bf16 bf16x2;
typedef __attribute__((ext_vector_type(4))) short s16x4;
typedef __attribute__((ext_vector_type(8))) short s16x8;
typedef __attribute__((ext_vector_type(16))) float f32x16;
typedef __attribute__((ext_vector_type(4))) float f32x4;
typedef __attribute__((ext_vector_type(4))) u32 u32x4;
typedef __attribute__((ext_vector_type(2))) u32 u32x2;
typedef __attribute__((address_space(3))) s16x4* lds_s16x4;
typedef __attribute__((address_space(3))) u32x4* lds_u32x4;
typedef __attribute__((address_space(3))) u32x2* lds_u32x2;
typedef __amdgpu_buffer_rsrc_t rsrc_t;

constexpr int S_ = 2048, D_ = 576, F_ = 4608;
constexpr u32 SLOT = 36864;
constexpr u32 XBYTES = S_ * D_ * 2;
constexpr u32 WBYTES = D_ * F_ * 2;
constexpr u32 PBYTES = 16u * 144u * 2048u * 8u;
constexpr u32 WS_KVT = PBYTES;
constexpr u32 WS_ST = WS_KVT + XBYTES;
constexpr u32 WS_XG = WS_ST + S_ * 8;

template <int V> struct IC { static constexpr int v = V; };
template <int... Is, class Fn> __device__ __forceinline__ void sfor_impl(std::integer_sequence<int, Is...>, Fn&& f) { (f(IC<Is>{}), ...); }
template <int N, class Fn> __device__ __forceinline__ void sfor(Fn&& f) { sfor_impl(std::make_integer_sequence<int, N>{}, f); }

__device__ __forceinline__ rsrc_t mk_rsrc(const void* p, u32 bytes) { return __builtin_amdgcn_make_buffer_rsrc((void*)p, (short)0, (int)bytes, 0x00020000); }
__device__ __forceinline__ s16x4 trrd(u32 addr) { return __builtin_amdgcn_ds_read_tr16_b64_v4i16((lds_s16x4)addr); }
__device__ __forceinline__ float bf_lo(u32 w) { return __uint_as_float(w << 16); }
__device__ __forceinline__ float bf_hi(u32 w) { return __uint_as_float(w & 0xffff0000u); }
__device__ __forceinline__ u32 pk_bf16(float a, float b) { bf16x2 r = {(__bf16)a, (__bf16)b}; return __builtin_bit_cast(u32, r); }

__device__ __forceinline__ void a_cvt(u32& r, float a, float b) { asm volatile("v_cvt_pk_bf16_f32 %0, %1, %2" : "=v"(r) : "v"(a), "v"(b)); }
__device__ __forceinline__ void a_mul(float& r, float a, float b) { asm volatile("v_mul_f32 %0, %1, %2" : "=v"(r) : "v"(a), "v"(b)); }
__device__ __forceinline__ void a_add1(float& r) { asm volatile("v_add_f32 %0, 1.0, %0" : "+v"(r)); }
__device__ __forceinline__ void a_exp(float& r, float a) { asm volatile("v_exp_f32 %0, %1" : "=v"(r) : "v"(a)); }
__device__ __forceinline__ void a_addrcp(float& r) { asm volatile("v_add_f32 %0, 1.0, %0\n\tv_rcp_f32 %0, %0" : "+v"(r)); }
template <int R> __device__ __forceinline__ void a_cvtwr(float x, float y) { u32 t; asm volatile("v_cvt_pk_bf16_f32 %0, %1, %2\n\tv_accvgpr_write_b32 a[%3], %0" : "=&v"(t) : "v"(x), "v"(y), "i"(R)); }
__device__ __forceinline__ u32 opaque(u32 x) { asm volatile("" : "+v"(x)); return x; }
__device__ __forceinline__ void a_norm4(u32x4& x, u32 clo, u32 chi, float d) {
  u32 x0 = x[0], x1 = x[1], x2 = x[2], x3 = x[3]; float t0, t1, t2, t3, t4, t5, t6, t7;
  asm volatile("v_dot2_f32_bf16 %4, %0, %12, %14\n\tv_dot2_f32_bf16 %5, %0, %13, %14\n\tv_dot2_f32_bf16 %6, %1, %12, %14\n\tv_dot2_f32_bf16 %7, %1, %13, %14\n\t"
               "v_dot2_f32_bf16 %8, %2, %12, %14\n\tv_dot2_f32_bf16 %9, %2, %13, %14\n\tv_dot2_f32_bf16 %10, %3, %12, %14\n\tv_dot2_f32_bf16 %11, %3, %13, %14\n\t"
               "v_cvt_pk_bf16_f32 %0, %4, %5\n\tv_cvt_pk_bf16_f32 %1, %6, %7\n\tv_cvt_pk_bf16_f32 %2, %8, %9\n\tv_cvt_pk_bf16_f32 %3, %10, %11"
               : "+v"(x0), "+v"(x1), "+v"(x2), "+v"(x3), "=&v"(t0), "=&v"(t1), "=&v"(t2), "=&v"(t3), "=&v"(t4), "=&v"(t5), "=&v"(t6), "=&v"(t7)
               : "v"(clo), "v"(chi), "v"(d));
  x[0] = x0; x[1] = x1; x[2] = x2; x[3] = x3;
}
__device__ __forceinline__ void a_rcp(float& r) { asm volatile("v_rcp_f32 %0, %0" : "+v"(r)); }
__device__ __forceinline__ void a_wr(u32& a, u32 v) { asm volatile("v_accvgpr_write_b32 %0, %1" : "=a"(a) : "v"(v)); }
__device__ __forceinline__ void a_swap16(u32& a, u32& b) { asm volatile("v_permlane16_swap_b32 %0, %1" : "+v"(a), "+v"(b)); }
__device__ __forceinline__ void mfma_v(f32x16& c, const bf16x8& a, const bf16x8& b) { asm volatile("v_mfma_f32_32x32x16_bf16 %0, %1, %2, %0" : "+v"(c) : "v"(a), "v"(b)); }
__device__ __forceinline__ void mfma_vz(f32x16& c, const bf16x8& a, const bf16x8& b) { asm volatile("v_mfma_f32_32x32x16_bf16 %0, %1, %2, 0" : "=&v"(c) : "v"(a), "v"(b)); }
template <int R> __device__ __forceinline__ void mfma_a(f32x16& c, const bf16x8& a) { asm volatile("v_mfma_f32_32x32x16_bf16 %0, %1, a[%2:%3], %0" : "+v"(c) : "v"(a), "i"(R), "i"(R + 3)); }
template <int R> __device__ __forceinline__ void mfma_az(f32x16& c, const bf16x8& a) { asm volatile("v_mfma_f32_32x32x16_bf16 %0, %1, a[%2:%3], 0" : "=&v"(c) : "v"(a), "i"(R), "i"(R + 3)); }
__device__ __forceinline__ void set_m0(u32 base, int imm) { asm volatile("s_add_u32 m0, %0, %1" :: "s"(base), "i"(imm) : "scc"); }
__device__ __forceinline__ void dma16(u32 voff, const rsrc_t& srd, u32 soff) { asm volatile("buffer_load_dwordx4 %0, %1, %2 offen lds" :: "v"(voff), "s"(srd), "s"(soff) : "memory"); }
template <int N> __device__ __forceinline__ void wait_vm() { asm volatile("s_waitcnt vmcnt(%0)" :: "i"(N) : "memory"); }
constexpr int wc_imm(int vm, int lgkm) { return (vm & 15) | (7 << 4) | (lgkm << 8) | ((vm >> 4) << 14); }
#define TX 1
#define K2X 96
#define PW1 -1
#define PW2 21
#define PHK 24
#define PC0 -1
#define PC1 27
#define PC2 30
#define PC3 33
#define STAMP(k)
#define STAMP_END
#define STAMP_DECL
#define RSTAMP(k)
#define DBGARG
#define STMOD "sc1"
constexpr u32 OOB = 0x80000000u;
#define PW2B 99
#define PCE 0
#define PKV 99
constexpr int TN_W = 1, TN_W1 = 1, TN_X = 1;
constexpr int touches(int lo, int hi) {
  return (PW1 >= lo && PW1 <= hi ? TN_W1 : 0) + (PW2 >= lo && PW2 <= hi ? TN_W : 0) + (PW2B >= lo && PW2B <= hi && PW2B != 99 ? TN_W : 0) + (PKV >= lo && PKV <= hi ? 2 * TN_X : 0) + (PHK >= lo && PHK <= hi ? 2 : 0)
    + (PC0 >= lo && PC0 <= hi ? 1 : 0) + (PC1 >= lo && PC1 <= hi ? 1 : 0) + (PC2 >= lo && PC2 <= hi ? 1 : 0) + (PC3 >= lo && PC3 <= hi ? 1 : 0);
}
constexpr int NSTEP = 18;
constexpr bool kv_slot(int n, int s) { return n >= 1 && n <= 7 && (s == 28 || s == 31 || s == 34); }
constexpr int KV_N0 = 12, KV_OPS = 68;
constexpr int kv_first(int n, int s) { return n < KV_N0 ? 0 : ((n - KV_N0) * 36 + s) * (4 * KV_OPS) / 215; }
constexpr int kv_stores(int n, int s) { return n < KV_N0 || s >= 36 ? 0 : (kv_first(n, s + 1) < 4 * KV_OPS ? kv_first(n, s + 1) : 4 * KV_OPS) / KV_OPS - kv_first(n, s) / KV_OPS; }
constexpr int nvm(int n, int s) {
  int c = 0;
  if (n <= 14 && s % 3 == 2 && s / 3 < 9) c += 1;
  if (n >= 10 && s < 36 && (s % 18 == 9 || s % 18 == 14)) c += 1;
  if ((n == 9 || n == 17) && s == 36) c += 2;
  if (kv_slot(n, s)) c += 1;
  c += kv_stores(n, s);
  if (s == 1) c += touches(20 + n, 20 + n);
  return c;
}
constexpr int vm_wait(int n) {
  int c = 0;
  if (n == 0) c = 9 + 1 + 3 + touches(17, 18);
  else if (n == 1) c = 1 + 3 + touches(18, 18);
  else for (int s = 27; s <= 36; ++s) c += nvm(n - 2, s);
  if (n >= 1) for (int s = 0; s <= 36; ++s) c += nvm(n - 1, s);
  for (int s = 0; s < 33; ++s) c += nvm(n, s);
  return c;
}
constexpr int eslot(int n, int v) { return 1 + 2 * v - (n == 9 ? (v >= 12 ? 2 : (v >= 8 ? 1 : 0)) : 0); }
constexpr int ord9(int s) { return s < 16 ? s : (s < 32 ? s + 2 : (s == 32 ? 16 : (s == 33 ? 34 : (s == 34 ? 17 : 35)))); }
constexpr int piece_of(int n, int s) { return n == 9 ? ord9(s) : s; }

__device__ __forceinline__ void vstore16(const u32x4& d, u32 voff, const rsrc_t& srd, u32 soff) { asm volatile("buffer_store_dwordx4 %0, %1, %2, %3 offen " STMOD :: "v"(d), "v"(voff), "s"(srd), "s"(soff) : "memory"); }
__device__ __forceinline__ void vstore8(const u32x2& d, u32 voff, const rsrc_t& srd) { asm volatile("buffer_store_dwordx2 %0, %1, %2, 0 offen sc1" :: "v"(d), "v"(voff), "s"(srd) : "memory"); }
template <int IMM> __device__ __forceinline__ void vload4(u32& d, u32 voff, const rsrc_t& srd) { asm volatile("buffer_load_dword %0, %1, %2, 0 offen offset:%3" : "=&v"(d) : "v"(voff), "s"(srd), "i"(IMM) : "memory"); }
template <int IMM> __device__ __forceinline__ void vload8(u32x2& d, u32 voff, const rsrc_t& srd) { asm volatile("buffer_load_dwordx2 %0, %1, %2, 0 offen offset:%3" : "=&v"(d) : "v"(voff), "s"(srd), "i"(IMM) : "memory"); }
template <int IMM> __device__ __forceinline__ void vload16(u32x4& d, u32 voff, const rsrc_t& srd) { asm volatile("buffer_load_dwordx4 %0, %1, %2, 0 offen offset:%3" : "=&v"(d) : "v"(voff), "s"(srd), "i"(IMM) : "memory"); }
__device__ __forceinline__ void dma16_m0(u32 m0v, u32 voff, const rsrc_t& srd, u32 soff) { asm volatile("s_mov_b32 m0, %0\n\ts_nop 0\n\tbuffer_load_dwordx4 %1, %2, %3 offen lds" :: "s"(m0v), "v"(voff), "s"(srd), "s"(soff) : "memory"); }
template <int R, int IMM> __device__ __forceinline__ void aload8(u32 voff, const rsrc_t& srd, u32 soff) { asm volatile("buffer_load_dwordx2 a[%0:%1], %2, %3, %4 offen offset:%5" :: "i"(R), "i"(R + 1), "v"(voff), "s"(srd), "s"(soff), "i"(IMM) : "memory"); }
template <int R, int IMM> __device__ __forceinline__ void aload16(u32 voff, const rsrc_t& srd, u32 soff) { asm volatile("buffer_load_dwordx4 a[%0:%1], %2, %3, %4 offen offset:%5" :: "i"(R), "i"(R + 3), "v"(voff), "s"(srd), "s"(soff), "i"(IMM) : "memory"); }
template <int R> __device__ __forceinline__ void a_rd(u32& d) { asm volatile("v_accvgpr_read_b32 %0, a[%1]" : "=v"(d) : "i"(R)); }
template <class T> __device__ __forceinline__ void tie_v(T& x) { asm volatile("" : "+v"(x)); }
__device__ __forceinline__ float dot2(u32 a, u32 b, float c) { return __builtin_amdgcn_fdot2_f32_bf16(__builtin_bit_cast(bf16x2, a), __builtin_bit_cast(bf16x2, b), c, false); }
__device__ __forceinline__ void mfma_vc(f32x16& d, const bf16x8& a, const bf16x8& b, const f32x16& c) { asm volatile("v_mfma_f32_32x32x16_bf16 %0, %1, %2, %3" : "=&v"(d) : "v"(a), "v"(b), "v"(c)); }
__device__ __forceinline__ void a_lo(float& r, u32 x) { asm volatile("v_lshlrev_b32 %0, 16, %1" : "=v"(r) : "v"(x)); }
__device__ __forceinline__ void a_hi(float& r, u32 x) { asm volatile("v_and_b32 %0, 0xffff0000, %1" : "=v"(r) : "v"(x)); }
__device__ __forceinline__ void a_fma(float& r, float a, float b) { asm volatile("v_fmac_f32 %0, %1, %2" : "+v"(r) : "v"(a), "v"(b)); }
__device__ __forceinline__ void a_swap32(float& a, float& b) { asm volatile("s_nop 1\n\tv_permlane32_swap_b32 %0, %1\n\ts_nop 1" : "+v"(a), "+v"(b)); }
__device__ __forceinline__ bf16x8 cat_frag(const s16x4& lo, const s16x4& hi) { return __builtin_bit_cast(bf16x8, __builtin_shufflevector(lo, hi, 0, 1, 2, 3, 4, 5, 6, 7)); }

__global__ __launch_bounds__(256, 1) void k1(const u16* __restrict__ xq, const u16* __restrict__ kin, const u16* __restrict__ vin, const u16* __restrict__ shk,
                                             const u16* __restrict__ w1, const u16* __restrict__ w2, const u16* __restrict__ hk, const u16* __restrict__ dv, char* __restrict__ ws DBGARG) {
  extern __shared__ __attribute__((aligned(16))) char smem[];
  const int lane = threadIdx.x & 63, w = __builtin_amdgcn_readfirstlane(threadIdx.x >> 6);
  const int b = blockIdx.x, xc = b & 7, lc = b >> 3;
  const int j = (xc >> 2) * 8 + (lc & 7), ib = (xc & 3) * 4 + (lc >> 3);
  if constexpr (PCE) {
    unsigned long long pc; u32 len;
    asm volatile("s_getpc_b64 %0\n.Lpf%=:\n\ts_mov_b32 %1, .Lk2_code_end-.Lpf%=" : "=s"(pc), "=s"(len));
      len = len < 65536u ? len : 0u;
    const u32 o = lane < 16 ? (u32)(w * 16 + lane) * 128 : (lane < 19 ? (u32)(lc * 4 + w + 128 * (lane - 16)) * 128 : OOB);
    asm volatile("buffer_load_dword a118, %0, %1, 0 offen" :: "v"(o), "s"(mk_rsrc((const void*)pc, len)) : "a118", "memory");
    __builtin_amdgcn_sched_barrier(0);
  }
  const u32 lds0 = (u32)(size_t)(lds_ptr_t)smem;
  const rsrc_t sX = mk_rsrc(xq, XBYTES), sW1 = mk_rsrc(w1, WBYTES);
  const int rowbase = ib * 128 + w * 32;
  STAMP_DECL;
  RSTAMP(0);
  STAMP(0);
  const int rho = lane >> 2;
  const u32 ldsw = lds0 + w * 9216;
  u32 tch0;
  vload4<0>(tch0, (TX && lane < 36) ? (u32)(ib * 147456 + ((lc & 7) * 144 + w * 36 + lane) * 128) : OOB, sX);
  const u32 xv0 = (u32)((rowbase + 2 * rho) * 1152 + (((lane & 3) ^ ((rho >> 2) & 3)) << 4)), xv1 = xv0 + 1152;
  auto xdma = [&](auto KC) { constexpr int kc = KC.v;
    dma16_m0(ldsw + (2 * (kc / 9)) * SLOT + (kc % 9) * 1024, xv0, sX, kc * 64);
    dma16_m0(ldsw + (2 * (kc / 9) + 1) * SLOT + (kc % 9) * 1024, xv1, sX, kc * 64); };
  constexpr int PD = 6;
  sfor<PD>(xdma);
  STAMP(1);
  const rsrc_t sK = mk_rsrc(kin, XBYTES), sV = mk_rsrc(vin, XBYTES), sS = mk_rsrc(shk, D_ * 8);
  const rsrc_t sW2 = mk_rsrc(w2, WBYTES), sWS = mk_rsrc(ws, WS_XG + XBYTES);
  auto touch1 = [&](u32 voff, const rsrc_t& srd, u32 soff) { asm volatile("buffer_load_dword a118, %0, %1, %2 offen" :: "v"(voff), "s"(srd), "s"(soff) : "a118", "memory"); };
  auto touch = [&](auto POS) { constexpr int pos = POS.v;
    if constexpr (PW1 == pos) {
      const u32 l = opaque((u32)lane), i = l + 45 * w, r = (i * 13108) >> 16;
      touch1(l < 45 ? r * (9216 - 640) + i * 128 : OOB, sW1, (u32)(36 * ib * 9216 + ((576 * j) & ~127))); }
    if constexpr (PKV == pos) { const u32 l = opaque((u32)lane), o = l < 18 ? (l + 18 * w) * 128 : OOB;
      touch1(o, sK, (u32)(ib * 147456 + j * 9216)); touch1(o, sV, (u32)(ib * 147456 + j * 9216)); }
    if constexpr (PW2 == pos || PW2B == pos) { constexpr u32 nl = PW2B == 99 ? 41 : 21; const u32 l = opaque((u32)lane), i = l + nl * w + (PW2 == pos ? 0 : 84);
      touch1(l < nl && i < 162 ? i * 128 : OOB, sW2, (u32)((j * 16 + ib) * 20736)); }
    if constexpr (PC0 == pos || PC1 == pos || PC2 == pos || PC3 == pos) {
      unsigned long long pc; u32 len;
      asm volatile("s_getpc_b64 %0\n.Lpf%=:\n\ts_mov_b32 %1, .Lk2_code_end-.Lpf%=" : "=s"(pc), "=s"(len));
      len = len < 65536u ? len : 0u;
      const u32 l = opaque((u32)lane);
      touch1(l < 3 ? (u32)(lc * 4 + w + 128 * l) * 128 : OOB, mk_rsrc((const void*)pc, len), 0u); }
    if constexpr (PHK == pos) { const u32 l = opaque((u32)lane);
      touch1(l < 18 ? (l + 18 * w) * 128 : OOB, mk_rsrc(hk, XBYTES), (u32)(b * 9216));
      touch1(l < 9 && b == 0 && w == 0 ? l * 128 : OOB, mk_rsrc(dv, D_ * 2), 0u); }
  };
  touch(IC<-1>{});
  const u32 v1 = (u32)(rho * 9216 + (lane & 3) * 16);
  const u32 s1 = __builtin_amdgcn_readfirstlane((u32)(144 * w * 9216 + 576 * j));
  const u32 s2 = __builtin_amdgcn_readfirstlane((u32)((288 * j + 144 * (w & 1)) * 1152 + (w >> 1) * 64));
  const int cl = lane & 31, h = lane >> 5, g = lane >> 4, q = (lane & 15) >> 2, p4 = lane & 3;
  bf16x8 RA[36];
  u32x4 X[36];
  f32x16 S1;
  float sq[2] = {0.f, 0.f};
  u32x4 xgd[3];
  {
    const int xr = cl & 15, sw = (xr >> 2) & 3;
    const u32 xa = ldsw + (cl >> 4) * SLOT + xr * 64;
    const u32 xa0 = opaque(xa + ((h ^ sw) << 4)), xa1 = opaque(xa + (((2 + h) ^ sw) << 4));
    const u32 xa0b = opaque(xa0 + 2 * SLOT), xa1b = opaque(xa1 + 2 * SLOT);
    const int tc = (g & 1) * 2 + (p4 >> 1), ts = 2 * (g >> 1);
    const u32 tb = ldsw + (8 * (g >> 1) + q) * 64 + (p4 & 1) * 8;
    u32 ta[4][2];
    sfor<4>([&](auto I) { ta[I.v][0] = opaque(tb + I.v * SLOT + ((tc ^ ts) << 4)); ta[I.v][1] = opaque(tb + I.v * SLOT + 256 + ((tc ^ (ts + 1)) << 4)); });
    u32x4 ones4 = {0x3f803f80u, 0x3f803f80u, 0x3f803f80u, 0x3f803f80u};
    asm volatile("s_nop 1" : "+v"(ones4));
    const bf16x8 ones = __builtin_bit_cast(bf16x8, ones4);
    u32x4 F[2][2];
    auto stat = [&](auto KC) { constexpr int kc = KC.v;
      sfor<2>([&](auto K) { constexpr int s = K.v;
        if constexpr (kc == 0 && s == 0) mfma_vz(S1, ones, __builtin_bit_cast(bf16x8, F[kc & 1][s])); else mfma_v(S1, ones, __builtin_bit_cast(bf16x8, F[kc & 1][s]));
        sfor<4>([&](auto E) { constexpr int e = E.v; sq[e & 1] = dot2(F[kc & 1][s][e], F[kc & 1][s][e], sq[e & 1]); }); }); };
    auto xg = [&](bool first) {
      const int gs = lane >> 4, rh = lane & 15;
      sfor<3>([&](auto I) { constexpr int i = I.v;
        const int gi = 9 * j + 4 * i + (i == 2 ? 0 : gs), kk = gi >> 3;
        const u32 ad = ldsw + (u32)((kk >= 9 ? 2 * SLOT - 9216 : 0) + kk * 1024 + rh * 64 + ((((gi & 7) >> 1) ^ ((rh >> 2) & 3)) << 4) + (gi & 1) * 8);
        const u32x2 e = *(lds_u32x2)ad, o = *(lds_u32x2)(ad + SLOT);
        const u32x4 d = {e[0], e[1], o[0], o[1]};
        if (first) xgd[i] = d; else xgd[i] = j < 8 ? xgd[i] : d; }); };
    sfor<18>([&](auto KC) { constexpr int kc = KC.v;
      if constexpr (kc + PD < 18) xdma(IC<kc + PD>{});
      else sfor<18 / PD>([&](auto I) { constexpr int pi = (kc + PD - 18) * (18 / PD) + I.v, n = pi / 9, p = pi % 9;
        dma16_m0(ldsw + n * SLOT + p * 1024, v1 + 64 * n, sW1, s1 + p * 147456); });
      touch(KC);
      wait_vm<((kc + PD < 18) ? 2 * PD : 2 * (17 - kc) + (kc + PD - 17) * (18 / PD)) + touches(kc - PD, kc)>();
      if constexpr (kc == 0) tie_v(tch0);
      constexpr u32 off = (kc % 9) * 1024;
      sfor<2>([&](auto R) { constexpr int rh = R.v, sl = 2 * (kc / 9) + rh; RA[2 * kc + rh] = cat_frag(trrd(ta[sl][0] + off), trrd(ta[sl][1] + off)); });
      F[kc & 1][0] = *(lds_u32x4)((kc < 9 ? xa0 : xa0b) + off); F[kc & 1][1] = *(lds_u32x4)((kc < 9 ? xa1 : xa1b) + off);
      if constexpr (kc >= 1) stat(IC<kc - 1>{});
      if constexpr (kc == 10) xg(true);
      __builtin_amdgcn_sched_barrier(0); });
    xg(false);
    STAMP(2);
    stat(IC<17>{});
    __builtin_amdgcn_s_waitcnt(wc_imm(63, 0));
    STAMP(4);
  }
  float sa = sq[0] + sq[1], sb = sa;
  a_swap32(sa, sb);
  asm volatile("s_nop 7" : "+v"(S1));
  const float mean = S1[0] * (1.f / 576.f);
  const float var = fmaxf((sa + sb) * (1.f / 576.f) - mean * mean, 0.f);
  const float rs = __builtin_amdgcn_rsqf(var + 1e-5f);
  {
    const int trow = rowbase + 2 * (cl & 15) + (cl >> 4);
    u32x2 st = {__float_as_uint(mean), __float_as_uint(rs)};
    const u32 cb = pk_bf16(-1.4426950408889634f * rs, 0.f) & 0xffffu;
    const float nmc = -mean * bf_lo(cb);
    u32x4 dg[2]; f32x16 C0;
    sfor<2>([&](auto R) { const int jj = cl - 16 * R.v - 8 * h; const u32 val = cb << ((jj & 1) * 16); const int je = jj >> 1;
      sfor<4>([&](auto E) { dg[R.v][E.v] = je == E.v ? val : 0u; }); });
    sfor<16>([&](auto I) { C0[I.v] = nmc; });
    asm volatile("s_nop 1" : "+v"(C0), "+v"(dg[0]), "+v"(dg[1]));
    const bf16x8 dg0 = __builtin_bit_cast(bf16x8, dg[0]), dg1 = __builtin_bit_cast(bf16x8, dg[1]);
    f32x16 Dn[3];
    auto cv = [&](auto KC, auto HF) { constexpr int kc = KC.v, hf = HF.v;
      sfor<4>([&](auto E) { constexpr int e = E.v; u32 t; a_cvt(t, Dn[kc % 3][8 * hf + 2 * e], Dn[kc % 3][8 * hf + 2 * e + 1]); X[2 * kc + hf][e] = t; }); };
    sfor<18>([&](auto KC) { constexpr int kc = KC.v;
      mfma_vc(Dn[kc % 3], RA[2 * kc], dg0, C0);
      if constexpr (kc >= 2) cv(IC<kc - 2>{}, IC<1>{});
      __builtin_amdgcn_sched_barrier(0);
      mfma_v(Dn[kc % 3], RA[2 * kc + 1], dg1);
      if constexpr (kc >= 1) cv(IC<kc - 1>{}, IC<0>{});
      if constexpr (kc % 2 == 0) dma16_m0(ldsw + 2 * SLOT + (kc / 2) * 1024, v1 + 128, sW1, s1 + (kc / 2) * 147456);
      __builtin_amdgcn_sched_barrier(0); });
    vstore8(st, (j == 0 && h == 0) ? (u32)(WS_ST + trow * 8) : OOB, sWS);
    { const int l2 = (int)opaque((u32)lane), gs = l2 >> 4;
      sfor<3>([&](auto I) { constexpr int i = I.v;
        vstore16(xgd[i], (i < 2 || gs == 0) ? (u32)(WS_XG + ((9 * j + 4 * i + gs) * 2048 + rowbase + 2 * (l2 & 15)) * 8) : OOB, sWS, 0u); }); }
    touch(IC<18>{});
    cv(IC<16>{}, IC<1>{});
    asm volatile("s_nop 7");
    cv(IC<17>{}, IC<0>{}); cv(IC<17>{}, IC<1>{});
  }
  STAMP(5);
  const int ln = (int)opaque((u32)lane);
  const u32 v2 = (u32)((ln >> 2) * 1152 + (ln & 3) * 16);
  const u32 a2 = lds0 + (4 * (ln >> 5) + ((ln & 15) >> 2)) * 64 + ((ln >> 4) & 1) * 32 + (ln & 3) * 8;
  u32 b2[4];
  sfor<4>([&](auto I) { b2[I.v] = opaque(a2 + I.v * SLOT); });
  const u32 pv0 = (u32)((2 * ((ln >> 4) & 1) + (ln >> 5)) * 16384 + (rowbase + 2 * (ln & 15)) * 8), pv1 = pv0 + 65536;
  u32 ps = (u32)j * (144u * 16384u);
  s16x4 A[6][2];
  f32x16 acc[3];
  asm volatile("" ::: "a0", "a117");
  float te[2], tm[2]; u32 pk[8];
  const int kq = ln % 9, ktg = ln / 9, kt0 = rowbase + 8 * ktg, kch = 36 * j + 4 * kq;
  const bool kact = ln < 36;
  const u32 kb = kact ? (u32)(kt0 * 1152 + kch * 2) : OOB, kbh = (kact && kt0 > 0) ? kb - 3456 : OOB, ksh = kact ? (u32)(kch * 8) : OOB;
  const u32 kst = kact ? (u32)(WS_KVT + kch * 4096 + kt0 * 2) : OOB;
  u32 kr[22], vr[16], sh[8];
  float kf[11], vf[8], kt[4], ko[8]; u32 kp[4];
  auto rdA = [&](auto N, auto POS, u32 base) { constexpr int pos = POS.v, pc = piece_of(N.v, pos);
    A[pos % 6][0] = trrd(base + pc * 1024); A[pos % 6][1] = trrd(base + pc * 1024 + 512); };
  wait_vm<18 + 3 + 1 + touches(14, 18)>();
  __builtin_amdgcn_s_barrier();
  sfor<4>([&](auto P) { rdA(IC<0>{}, P, b2[0]); });
  STAMP(6);
  sfor<NSTEP>([&](auto N) { constexpr int n = N.v;
    constexpr int sl = n & 3, sn = (n + 1) & 3, sd = (n + 3) & 3;
    sfor<36>([&](auto S) { constexpr int s = S.v;
      if constexpr (s % 3 == 0) __builtin_amdgcn_s_waitcnt(wc_imm(63, s == 33 ? 0 : 2));
      const bf16x8 av = cat_frag(A[s % 6][0], A[s % 6][1]);
      if constexpr (n < 9) {
        const bf16x8 bv = __builtin_bit_cast(bf16x8, X[s]);
        if constexpr (s == 0) mfma_vz(acc[n & 1], av, bv); else mfma_v(acc[n & 1], av, bv);
      } else {
        constexpr int pc = piece_of(n, s), hf = pc % 18, ac = 1 + pc / 18;
        if constexpr (hf == 0) mfma_az<0>(acc[ac], av); else mfma_a<4 * hf>(acc[ac], av);
      }
      if constexpr (s <= 31) rdA(N, IC<s + 4>{}, b2[sl]);
      if constexpr (n < NSTEP - 1) {
        if constexpr (s == 33) { wait_vm<vm_wait(n)>(); __builtin_amdgcn_s_barrier(); rdA(IC<n + 1>{}, IC<0>{}, b2[sn]); }
        if constexpr (s == 34) { rdA(IC<n + 1>{}, IC<1>{}, b2[sn]); rdA(IC<n + 1>{}, IC<2>{}, b2[sn]); }
        if constexpr (s == 35) rdA(IC<n + 1>{}, IC<3>{}, b2[sn]);
      }
      if constexpr (n >= 1 && n <= 9) {
        constexpr int pa = (n - 1) & 1;
        sfor<16>([&](auto V) { constexpr int v = V.v; if constexpr ((v & 1) && eslot(n, v) + 3 == s) a_cvtwr<8 * (n - 1) + (v >> 1)>(tm[0], tm[1]); });
        sfor<16>([&](auto V) { constexpr int v = V.v; if constexpr (eslot(n, v) + 2 == s) a_mul(tm[v & 1], acc[pa][v], te[v & 1]); });
        sfor<16>([&](auto V) { constexpr int v = V.v; if constexpr (eslot(n, v) + 1 == s) a_addrcp(te[v & 1]); });
        sfor<16>([&](auto V) { constexpr int v = V.v; if constexpr (eslot(n, v) == s) a_exp(te[v & 1], acc[pa][v]); });
      }
      if constexpr (n >= 10) {
        constexpr int hb = s < 18 ? 0 : 18, r = s - hb, ac = s < 18 ? 2 : 1;
        if constexpr (r >= 2 && r <= 9) { constexpr int k = r - 2; a_cvt(pk[k], acc[ac][2 * k], acc[ac][2 * k + 1]); }
        if constexpr (r == 7) a_swap16(pk[0], pk[2]);
        if constexpr (r == 8) a_swap16(pk[1], pk[3]);
        if constexpr (r == 9) { u32x4 d = {pk[0], pk[1], pk[2], pk[3]}; vstore16(d, pv0, sWS, ps); }
        if constexpr (r == 11) a_swap16(pk[4], pk[6]);
        if constexpr (r == 12) a_swap16(pk[5], pk[7]);
        if constexpr (r == 14) { u32x4 d = {pk[4], pk[5], pk[6], pk[7]}; vstore16(d, pv1, sWS, ps); ps += 131072; }
      }
      if constexpr (kv_slot(n, s)) {
        constexpr int l = 3 * (n - 1) + (s - 28) / 3;
        if constexpr (l < 3) aload8<72 + 2 * l, l * 1152>(kbh, sK, 0);
        else if constexpr (l < 7) aload8<72 + 2 * l, (l - 3) * 1152>(kb, sK, 0);
        else if constexpr (l < 11) aload8<72 + 2 * l, (l - 7) * 1152>(kb, sK, 4608);
        else if constexpr (l < 15) aload8<72 + 2 * l, (l - 11) * 1152>(kb, sV, 0);
        else if constexpr (l < 19) aload8<72 + 2 * l, (l - 15) * 1152>(kb, sV, 4608);
        else aload16<110 + 4 * (l - 19), (l - 19) * 16>(ksh, sS, 0);
      }
      if constexpr (n == 10) { if constexpr (s < 22) a_rd<72 + s>(kr[s]); else a_rd<72 + s>(vr[s - 22]); }
      if constexpr (n == 11 && s < 10) { if constexpr (s < 2) a_rd<108 + s>(vr[14 + s]); else a_rd<108 + s>(sh[s - 2]); }
      if constexpr (n >= KV_N0) {
        constexpr int u0 = kv_first(n, s), u1 = kv_first(n, s + 1) < 4 * KV_OPS ? kv_first(n, s + 1) : 4 * KV_OPS;
        sfor<u1 - u0>([&](auto I) { constexpr int c = (u0 + I.v) / KV_OPS, u = (u0 + I.v) % KV_OPS;
          if constexpr (u < 4) { if constexpr (u & 1) a_hi(kt[u], sh[2 * c + (u >> 1)]); else a_lo(kt[u], sh[2 * c + (u >> 1)]); }
          else if constexpr (u < 15) { if constexpr (c & 1) a_hi(kf[u - 4], kr[2 * (u - 4) + (c >> 1)]); else a_lo(kf[u - 4], kr[2 * (u - 4) + (c >> 1)]); }
          else if constexpr (u < 23) { if constexpr (c & 1) a_hi(vf[u - 15], vr[2 * (u - 15) + (c >> 1)]); else a_lo(vf[u - 15], vr[2 * (u - 15) + (c >> 1)]); }
          else if constexpr (u < 63) { constexpr int st = (u - 23) / 8, r = (u - 23) % 8;
            if constexpr (st == 0) a_mul(ko[r], kf[r + 3], kt[0]);
            else if constexpr (st < 4) a_fma(ko[r], kf[r + 3 - st], kt[st]);
            else a_mul(ko[r], ko[r], vf[r]); }
          else if constexpr (u < 67) a_cvt(kp[u - 63], ko[2 * (u - 63)], ko[2 * (u - 63) + 1]);
          else { u32x4 d = {kp[0], kp[1], kp[2], kp[3]}; vstore16(d, kst, sWS, c * 4096); } });
      }
      if constexpr (s == 1) touch(IC<20 + n>{});
      if constexpr (n <= 14) {
        if constexpr (s % 3 == 1 && s / 3 < 9) set_m0(ldsw, sd * SLOT + (s / 3) * 1024);
        if constexpr (s % 3 == 2 && s / 3 < 9) { constexpr int p = s / 3, t = n + 3;
          if constexpr (t < 9) dma16(v1 + 64 * t, sW1, s1 + p * 147456); else dma16(v2 + 128 * (t - 9), sW2, s2 + p * 18432); }
      }
      __builtin_amdgcn_sched_barrier(0);
    });
    if constexpr (n == 9 || n == NSTEP - 1) {
      constexpr int ac = n == 9 ? 1 : 2;
      asm volatile("s_nop 15" ::: "memory");
      sfor<8>([&](auto K) { constexpr int k = K.v; a_cvt(pk[k], acc[ac][2 * k], acc[ac][2 * k + 1]); });
      asm volatile("s_nop 1" ::: "memory");
      a_swap16(pk[0], pk[2]); a_swap16(pk[1], pk[3]); a_swap16(pk[4], pk[6]); a_swap16(pk[5], pk[7]);
      { u32x4 d = {pk[0], pk[1], pk[2], pk[3]}; vstore16(d, pv0, sWS, ps); }
      { u32x4 d = {pk[4], pk[5], pk[6], pk[7]}; vstore16(d, pv1, sWS, ps); ps += 131072; }
      __builtin_amdgcn_sched_barrier(0);
    }
    if constexpr (n == 0) STAMP(7); if constexpr (n == 8) STAMP(8); if constexpr (n == 9) STAMP(9); if constexpr (n == 14) STAMP(10); if constexpr (n == 17) { STAMP(11); RSTAMP(1); }
  });
  STAMP_END;
}
#define STAMP3(k)
#define STAMP3_DECL
#define STAMP3_END
#define DBGARG2
#define K2S 3
constexpr u32 K2_HG = 4160, K2_KR = 1152;
constexpr u32 K2_OKR = 4 * K2_HG, K2_OKV = K2_OKR + 4 * K2_KR, K2_OYS = K2_OKV + 4096, K2_CH = K2_OYS + 8192;
constexpr u32 K2_LDS = 4 * K2_CH;
typedef __attribute__((address_space(3))) u32* lds_u32;
typedef __attribute__((address_space(3))) f32x4* lds_f32x4;
typedef __attribute__((ext_vector_type(2))) float f32x2;
typedef __attribute__((address_space(3))) f32x2* lds_f32x2;
__device__ __forceinline__ u32x4 bload16(const rsrc_t& r, u32 v, u32 s) { return __builtin_amdgcn_raw_buffer_load_b128(r, (int)v, (int)s, 0); }
__device__ __forceinline__ u32x2 bload8(const rsrc_t& r, u32 v, u32 s) { return __builtin_amdgcn_raw_buffer_load_b64(r, (int)v, (int)s, 0); }

#define OAUX 0
__global__ __launch_bounds__(768, 1) void k2(const u16* __restrict__ xq, const u16* __restrict__ hk, const u16* __restrict__ dv,
                                             const char* __restrict__ ws, u16* __restrict__ out DBGARG2) {
  extern __shared__ __attribute__((aligned(16))) char smem[];
  const int lane = threadIdx.x & 63, w = __builtin_amdgcn_readfirstlane(threadIdx.x >> 6);
  const int dq = (blockIdx.x & 7) * 18 + (blockIdx.x >> 3);
  const u32 lds0 = (u32)(size_t)(lds_ptr_t)smem;
  STAMP3_DECL;
  STAMP3(0);
  if (w < 4 && (K2X & 1)) { __builtin_amdgcn_s_barrier(); } else
  if (w < 4) {
    if constexpr (K2X & 32) __builtin_amdgcn_s_setprio(2);
    const int d = 4 * dq + w;
    const u32 L = lds0 + w * K2_CH;
    const rsrc_t sH = mk_rsrc((K2X & 512) ? (const void*)(ws + WS_KVT + (size_t)((d + 7) % 576) * S_ * 2) : (const void*)(hk + (size_t)d * S_), S_ * 2), sKV = mk_rsrc(ws + WS_KVT + (size_t)d * S_ * 2, S_ * 2);
    u32x4 kc[4], ha[5], hb[5];
    sfor<4>([&](auto R) { kc[R.v] = bload16(sKV, (u32)(lane * 16), R.v * 1024); });
    sfor<5>([&](auto R) { const u32 o = (u32)((64 * R.v + lane - 2) * 16);
      ha[R.v] = bload16(sH, o, 0); hb[R.v] = bload16(sH, o + 16, 0); });
    const float dcoef = bf_lo(dv[d]);
    if constexpr (K2X & 64) { __builtin_amdgcn_sched_barrier(0); __builtin_amdgcn_s_barrier(); __builtin_amdgcn_sched_barrier(0); }
    if constexpr (K2S == 0) STAMP3(1);
    *(lds_u32x2)(L + K2_OKR + (lane >> 4) * K2_KR + 1024 + (lane & 15) * 8) = u32x2{0u, 0u};
    sfor<4>([&](auto R) { const int u = 64 * R.v + lane;
      *(lds_u32x4)(L + K2_OKV + u * 16) = kc[R.v];
      sfor<4>([&](auto Q) { constexpr int r = Q.v;
        const u32 lo = kc[R.v][2 + (r >> 1)], hi = kc[R.v][r >> 1];
        *(lds_u32)(L + K2_OKR + r * K2_KR + (510 - 2 * u) * 2) = __builtin_amdgcn_perm(hi, lo, (r & 1) ? 0x07060302u : 0x05040100u); }); });
    if constexpr (K2S == 1) STAMP3(1);
    if constexpr (K2S == 2) { __builtin_amdgcn_s_waitcnt(wc_imm(0, 15)); STAMP3(1); }
    sfor<5>([&](auto R) { const int v = 64 * R.v + lane;
      if ((R.v < 4 || lane < 2) && !(K2X & 256)) {
        const u32 W[8] = {ha[R.v][0], ha[R.v][1], ha[R.v][2], ha[R.v][3], hb[R.v][0], hb[R.v][1], hb[R.v][2], hb[R.v][3]};
        sfor<4>([&](auto G) { constexpr int g = G.v, s = g + 1; u32x4 o;
          sfor<4>([&](auto I) { constexpr int i = I.v;
            if constexpr (s & 1) o[i] = __builtin_amdgcn_alignbit(W[(s + 1) / 2 + i], W[(s - 1) / 2 + i], 16); else o[i] = W[s / 2 + i]; });
          if constexpr (K2X & 1024) { const u32x2 lo = {o[0], o[1]}, hi = {o[2], o[3]}; const u32 ad = L + v * 16;
            asm volatile("ds_write_b64 %0, %1 offset:%3\n\tds_write_b64 %0, %2 offset:%4" :: "v"(ad), "v"(lo), "v"(hi), "i"(g * K2_HG), "i"(g * K2_HG + 8) : "memory"); }
          else *(lds_u32x4)(L + g * K2_HG + v * 16) = o; }); } });
    if constexpr (K2S == 3) STAMP3(1);
    const int a0 = lane & 15, g = lane >> 4, q = a0 >> 2, p4 = lane & 3;
    u32 pa = L + g * K2_HG + (4 * q + 4 * p4) * 2;
    u32 pb = L + K2_OKR + (3 - g) * K2_KR + 1016 - 8 * a0 - 896;
    bf16x8 A[8], B[8]; f32x4 acc[8];
    auto ldA = [&](u32 base, auto K) { constexpr u32 o = K.v * 512; A[K.v] = __builtin_bit_cast(bf16x8, __builtin_shufflevector(trrd(base + o), trrd(base + o + 32), 0, 1, 2, 3, 4, 5, 6, 7)); };
    auto ldB = [&](u32 base, auto E) { constexpr u32 o = (7 - E.v) * 128; const u32x2 lo = *(lds_u32x2)(base + o), hi = *(lds_u32x2)(base + o + 8);
      B[E.v] = __builtin_bit_cast(bf16x8, u32x4{lo[0], lo[1], hi[0], hi[1]}); };
    sfor<8>([&](auto C) { acc[C.v] = f32x4{0.f, 0.f, 0.f, 0.f}; });
    sfor<8>([&](auto K) { ldA(pa, K); ldB(pb, IC<7 - K.v>{}); });
#pragma unroll 1
    for (int k0 = 0; k0 < ((K2X & 128) ? 1 : 8); ++k0) {
      pa += 64; pb += 16;
      sfor<8>([&](auto K1) { constexpr int k1 = K1.v;
        sfor<8 - k1>([&](auto I) { constexpr int i = I.v;
          constexpr int c = k1 == 0 ? (i == 0 ? 6 : (i == 1 ? 5 : (i == 2 ? 7 : 7 - i))) : 7 - i;
          acc[c] = __builtin_amdgcn_mfma_f32_16x16x32_bf16(A[k1], B[c - k1], acc[c], 0, 0, 0); });
        ldA(pa, K1); ldB(pb, IC<7 - k1>{});
        __builtin_amdgcn_sched_barrier(0); });
    }
    STAMP3(2);
    sfor<8>([&](auto C) { constexpr int c = C.v; const int t0 = 256 * c + 16 * a0 + 4 * g;
      const u32x2 kk = *(lds_u32x2)(L + K2_OKV + t0 * 2);
      f32x4 y = acc[c];
      y[0] += dcoef * bf_lo(kk[0]); y[1] += dcoef * bf_hi(kk[0]); y[2] += dcoef * bf_lo(kk[1]); y[3] += dcoef * bf_hi(kk[1]);
      *(lds_f32x4)(L + K2_OYS + t0 * 4) = y; });
    __builtin_amdgcn_s_waitcnt(wc_imm(63, 0));
    asm volatile("" ::: "memory");
    __builtin_amdgcn_s_barrier();
    STAMP3(3);
  } else {
    if constexpr (K2X & 16) __builtin_amdgcn_s_setprio(2);
    const int p = (w - 4) * 64 + lane;
    const rsrc_t sP = mk_rsrc(ws, PBYTES), sX = mk_rsrc(ws + WS_XG, XBYTES), sS = mk_rsrc(ws + WS_ST, S_ * 8), sO = mk_rsrc(out, XBYTES);
    const u32 pv = (u32)(dq * 16384 + p * 16), xv = (u32)(2 * p * 1152 + dq * 8);
    if constexpr (K2X & 64) { __builtin_amdgcn_sched_barrier(0); __builtin_amdgcn_s_barrier(); __builtin_amdgcn_sched_barrier(0); }
    u32x4 v[16]; u32x4 xr[2]; u32x4 st[2]; float sum[2][8];
    sfor<16>([&](auto J) { v[J.v] = bload16(sP, pv, J.v * (144 * 16384)); });
    xr[0] = bload16(sX, pv, 0); st[0] = bload16(sS, (u32)(p * 16), 0);
    u32 selo = 0x00003f80u, sehi = 0x3f800000u;
    asm volatile("" : "+v"(selo), "+v"(sehi));
    auto add = [&](auto R, auto J) { constexpr int r = R.v, j = J.v;
      sfor<4>([&](auto E) { constexpr int e = E.v;
        if constexpr (j == 0) { sum[r][2 * e] = bf_lo(v[j][e]); sum[r][2 * e + 1] = bf_hi(v[j][e]); }
        else if constexpr (K2X & 8) { sum[r][2 * e] += bf_lo(v[j][e]); sum[r][2 * e + 1] += bf_hi(v[j][e]); }
        else { sum[r][2 * e] = dot2(v[j][e], selo, sum[r][2 * e]); sum[r][2 * e + 1] = dot2(v[j][e], sehi, sum[r][2 * e + 1]); } }); };
    sfor<16>([&](auto J) { add(IC<0>{}, J); v[J.v] = bload16(sP, pv + 8192, J.v * (144 * 16384)); __builtin_amdgcn_sched_barrier(0); });
    xr[1] = bload16(sX, pv + 8192, 0); st[1] = bload16(sS, (u32)(p * 16), 8192);
    sfor<16>([&](auto J) { add(IC<1>{}, J); });
    STAMP3(1);
    __builtin_amdgcn_sched_barrier(0);
    __builtin_amdgcn_s_barrier();
    asm volatile("" ::: "memory");
    STAMP3(2);
    auto fin = [&](auto R) { constexpr int r = R.v;
      f32x2 y[4];
      sfor<4>([&](auto C) { y[C.v] = *(lds_f32x2)(lds0 + C.v * K2_CH + K2_OYS + (1024 * r + 2 * p) * 4); });
      sfor<2>([&](auto T) { constexpr int t = T.v;
        const float mean = __uint_as_float(st[r][2 * t]), rs = __uint_as_float(st[r][2 * t + 1]);
        float o[4];
        sfor<4>([&](auto C) { constexpr int c = C.v;
          const float xf = (c & 1) ? bf_hi(xr[r][2 * t + (c >> 1)]) : bf_lo(xr[r][2 * t + (c >> 1)]);
          const float z = sum[r][4 * t + c];
          const float gate = (-0.6931471805599453f * z) * __builtin_amdgcn_rcpf(1.f + __builtin_amdgcn_exp2f(z));
          o[c] = xf + (xf - mean) * rs * y[c][t] * gate; });
        u32x2 pk = {pk_bf16(o[0], o[1]), pk_bf16(o[2], o[3])};
        __builtin_amdgcn_raw_buffer_store_b64(pk, sO, (int)((K2X & 4) ? (u32)(dq * 16384 + p * 16 + t * 8) : xv + t * 1152), (K2X & 4) ? r * 8192 : r * 1024 * 1152, OAUX); }); };
    fin(IC<0>{});
    __builtin_amdgcn_sched_barrier(0);
    STAMP3(3);
    fin(IC<1>{});
  }
  STAMP3(4);
  STAMP3_END;
  asm volatile(".Lk2_code_end:");
}

extern "C" void kernel_launch(void* const* d_in, const int* in_sizes, int n_in, void* d_out, int out_size,
                              void* d_ws, size_t ws_size, hipStream_t stream) {
  (void)in_sizes; (void)n_in; (void)out_size; (void)ws_size;
  const u16* q = (const u16*)d_in[0]; const u16* k = (const u16*)d_in[1]; const u16* v = (const u16*)d_in[2];
  const u16* sk = (const u16*)d_in[3]; const u16* hk = (const u16*)d_in[4]; const u16* dv = (const u16*)d_in[5];
  const u16* w1 = (const u16*)d_in[7]; const u16* w2 = (const u16*)d_in[8];
  (void)hipFuncSetAttribute((const void*)k1, hipFuncAttributeMaxDynamicSharedMemorySize, 147456);
  (void)hipFuncSetAttribute((const void*)k2, hipFuncAttributeMaxDynamicSharedMemorySize, (int)K2_LDS);
  k1<<<256, 256, 147456, stream>>>(q, k, v, sk, w1, w2, hk, dv, (char*)d_ws);
  k2<<<144, 768, K2_LDS, stream>>>(q, hk, dv, (const char*)d_ws, (u16*)d_out);
}
```
